```python
import torch

def _softmax(x, axis=-1):
    return torch.softmax(x, dim=axis)
def _gelu(x):
    return torch.nn.functional.gelu(x, approximate="tanh")
def _silu(x):
    return torch.nn.functional.silu(x)
def _sigmoid(x):
    return torch.sigmoid(x)
def _layernorm(x, axis=-1, eps=1e-5):
    m = x.mean(dim=axis, keepdim=True)
    v = x.var(dim=axis, keepdim=True, unbiased=False)
    return (x - m) / torch.sqrt(v + eps)
def _scatter_add(tgt, idx, src, axis=0):
    return tgt.clone().index_add_(axis, idx, src)
def _segment_sum(src, seg, n):
    out = torch.zeros((n,) + src.shape[1:], dtype=src.dtype)
    return out.index_add_(0, seg, src)
def _pad(x, pw):
    flat = tuple(v for pair in reversed(pw) for v in pair)
    return torch.nn.functional.pad(x, flat)

kernel_name = 'gated_mix__grouped_kv_mqa__grouped_kv_mqa__prompt'

def setup_inputs():

    _w = (lambda a: (lambda t: t.to(torch.bfloat16) if t.is_floating_point() else t)(torch.as_tensor(a)))
    def _A_setup():
        B, S, D, H = 1, 2048, 512, 32
        Dh = D // H
        return {
            "x": _w((lambda *s: torch.randn(*s, dtype=torch.bfloat16) * 0.05)(B, S, D)),
            "wq": _w((lambda *s: torch.randn(*s, dtype=torch.bfloat16) * 0.8)(D, H * Dh)),
            "wk": _w((lambda *s: torch.randn(*s, dtype=torch.bfloat16) * 0.8)(D, Dh)),
            "wv": _w((lambda *s: torch.randn(*s, dtype=torch.bfloat16) * 0.05)(D, Dh)),
            "wo": _w((lambda *s: torch.randn(*s, dtype=torch.bfloat16) * 0.05)(H * Dh, D)),
        }
    _A_in = _A_setup()

    def _B_setup():
        B, S, D, H = 1, 2048, 512, 32
        Dh = D // H
        return {
            "x": _w((lambda *s: torch.randn(*s, dtype=torch.bfloat16) * 0.05)(B, S, D)),
            "wq": _w((lambda *s: torch.randn(*s, dtype=torch.bfloat16) * 0.8)(D, H * Dh)),
            "wk": _w((lambda *s: torch.randn(*s, dtype=torch.bfloat16) * 0.8)(D, Dh)),
            "wv": _w((lambda *s: torch.randn(*s, dtype=torch.bfloat16) * 0.05)(D, Dh)),
            "wo": _w((lambda *s: torch.randn(*s, dtype=torch.bfloat16) * 0.05)(H * Dh, D)),
        }
    _B_in = _B_setup()

    return {
        "A_x": _A_in["x"],
        "A_wq": _A_in["wq"],
        "A_wk": _A_in["wk"],
        "A_wv": _A_in["wv"],
        "A_wo": _A_in["wo"],
        "B_x": _B_in["x"],
        "B_wq": _B_in["wq"],
        "B_wk": _B_in["wk"],
        "B_wv": _B_in["wv"],
        "B_wo": _B_in["wo"],
        "gate_w": _w((lambda *s: torch.randn(*s, dtype=torch.bfloat16) * 0.05)(512) * 16.0),
    }

def reference(A_x, A_wq, A_wk, A_wv, A_wo, B_x, B_wq, B_wk, B_wv, B_wo, gate_w):
    def _A_ref(x, wq, wk, wv, wo):
        B, S, D = x.shape
        H = 32
        Dh = D // H
        q = (lambda x, *d: x.permute(*d))((x @ wq).reshape(B, S, H, Dh), 0, 2, 1, 3)
        k = x @ wk
        v = x @ wv
        scores = torch.einsum("bhqd,bkd->bhqk", q, k) * (Dh ** -0.5)
        p = _softmax(scores, axis=-1)
        o = torch.einsum("bhqk,bkd->bhqd", p, v)
        return (lambda x, *d: x.permute(*d))(o, 0, 2, 1, 3).reshape(B, S, H * Dh) @ wo

    def _B_ref(x, wq, wk, wv, wo):
        B, S, D = x.shape
        H = 32
        Dh = D // H
        q = (lambda x, *d: x.permute(*d))((x @ wq).reshape(B, S, H, Dh), 0, 2, 1, 3)
        k = x @ wk
        v = x @ wv
        scores = torch.einsum("bhqd,bkd->bhqk", q, k) * (Dh ** -0.5)
        p = _softmax(scores, axis=-1)
        o = torch.einsum("bhqk,bkd->bhqd", p, v)
        return (lambda x, *d: x.permute(*d))(o, 0, 2, 1, 3).reshape(B, S, H * Dh) @ wo

    x = A_x
    g = _sigmoid((x * gate_w).sum(-1, keepdims=True))
    a = _A_ref(x, A_wq, A_wk, A_wv, A_wo)
    b = _B_ref(x, B_wq, B_wk, B_wv, B_wo)
    return g * a + (1 - g) * b
```

```cpp
#include <hip/hip_runtime.h>
#include <hip/hip_bf16.h>
#include <stdint.h>


typedef float f32x16 __attribute__((ext_vector_type(16)));
typedef float f32x8 __attribute__((ext_vector_type(8)));
typedef float f32x4 __attribute__((ext_vector_type(4)));
typedef __bf16 bf16x8 __attribute__((ext_vector_type(8)));
typedef __bf16 bf16x16 __attribute__((ext_vector_type(16)));
typedef __bf16 bf16x2 __attribute__((ext_vector_type(2)));
typedef float f32x2 __attribute__((ext_vector_type(2)));
typedef short s16x4 __attribute__((ext_vector_type(4)));
typedef unsigned u32x4 __attribute__((ext_vector_type(4)));
typedef unsigned u32x2 __attribute__((ext_vector_type(2)));
typedef unsigned short u16;
typedef unsigned long long u64;
#define LAS __attribute__((address_space(3)))
#define GAS __attribute__((address_space(1)))
#define DI __device__ __forceinline__
#define RLX_AGENT __ATOMIC_RELAXED, __HIP_MEMORY_SCOPE_AGENT
#define RLX_WG __ATOMIC_RELAXED, __HIP_MEMORY_SCOPE_WORKGROUP
typedef LAS char* ldsp;

constexpr int DM = 512;
constexpr size_t WS_KV = 0;
constexpr size_t WS_EPOCH = 262144;
constexpr size_t WS_KVFLAG = 262400;
constexpr size_t WS_OFLAG = 266496;
constexpr size_t WS_O = 327680;
constexpr float QSCALE = 0.25f * 1.4426950408889634f;
constexpr float FAST_BOUND = 40.f;
constexpr unsigned SPIN_CAP = 1u << 20;

constexpr int F_WKV = 131072, F_GW = 122880;
constexpr int slot_of(int c) { return c == 2 ? 0 : c == 0 || c == 6 ? 24576 : c == 1 || c == 7 ? 49152 : c == 3 ? 73728 : c == 4 ? 98304 : F_WKV; }
constexpr int M_ONES = 131072, M_WO = 143360, M_CNT = 162432, M_STAT = 162464, M_Z = 162560;
constexpr int T_RED = 65536;
constexpr int LDS_BYTES = 163840;

DI unsigned cvtpk(float lo, float hi) { f32x2 v = {lo, hi}; return __builtin_bit_cast(unsigned, __builtin_convertvector(v, bf16x2)); }
DI float bflo(unsigned w) { return __uint_as_float(w << 16); }
DI float bfhi(unsigned w) { return __uint_as_float(w & 0xffff0000u); }
DI float sq2(unsigned w) { return bflo(w) * bflo(w) + bfhi(w) * bfhi(w); }
DI f32x16 mfma32(u32x4 a, u32x4 b, f32x16 c) { return __builtin_amdgcn_mfma_f32_32x32x16_bf16(__builtin_bit_cast(bf16x8, a), __builtin_bit_cast(bf16x8, b), c, 0, 0, 0); }
DI f32x4 mfma16(u32x4 a, u32x4 b, f32x4 c) { return __builtin_amdgcn_mfma_f32_16x16x32_bf16(__builtin_bit_cast(bf16x8, a), __builtin_bit_cast(bf16x8, b), c, 0, 0, 0); }
DI u32x2 tr_read(ldsp p) { return __builtin_bit_cast(u32x2, __builtin_amdgcn_ds_read_tr16_b64_v4i16((LAS s16x4*)p)); }
DI u32x4 tr_read2(ldsp p0, ldsp p1) { const u32x2 a = tr_read(p0), b = tr_read(p1); return (u32x4){a[0], a[1], b[0], b[1]}; }
DI u32x4 lds_b128(ldsp p) { return *(LAS const u32x4*)p; }
DI f32x4 lds_f128(ldsp p) { return *(LAS const f32x4*)p; }
DI void glds(const void* base, unsigned voff, unsigned lds_dst) {
    asm volatile("s_mov_b32 m0, %2\n\ts_nop 0\n\tglobal_load_lds_dwordx4 %0, %1" :: "v"(voff), "s"(base), "s"(lds_dst) : "memory");
}
DI void glds_dev(const void* base, unsigned voff, unsigned lds_dst) {
    asm volatile("s_mov_b32 m0, %2\n\ts_nop 0\n\tglobal_load_lds_dwordx4 %0, %1 sc1" :: "v"(voff), "s"(base), "s"(lds_dst) : "memory");
}
DI void lds_max(unsigned lds_addr, unsigned v) { asm volatile("ds_max_u32 %0, %1" :: "v"(lds_addr), "v"(v) : "memory"); }
DI void store16_dev(void* p, u32x4 v) { asm volatile("global_store_dwordx4 %0, %1, off sc1\n\ts_nop 1" :: "v"(p), "v"(v) : "memory"); }
#define WAIT_BAR(N) asm volatile("s_waitcnt vmcnt(" #N ") lgkmcnt(0)\n\ts_barrier" ::: "memory")
#define WAIT_VM(N) asm volatile("s_waitcnt vmcnt(" #N ")" ::: "memory")
#define LDS_BAR() asm volatile("s_waitcnt lgkmcnt(0)\n\ts_barrier" ::: "memory")
DI unsigned xor32u(unsigned v) { auto r = __builtin_amdgcn_permlane32_swap(v, v, false, false); return (threadIdx.x & 32) ? r[0] : r[1]; }
DI unsigned xor16u(unsigned v) { auto r = __builtin_amdgcn_permlane16_swap(v, v, false, false); return (threadIdx.x & 16) ? r[0] : r[1]; }
DI float xor32(float v) { return __uint_as_float(xor32u(__float_as_uint(v))); }
DI float xor16(float v) { return __uint_as_float(xor16u(__float_as_uint(v))); }
DI float max16(float v) {
    asm volatile("s_nop 1\n\tv_max_f32_dpp %0, %0, %0 row_ror:1 row_mask:0xf bank_mask:0xf\n\ts_nop 1\n\tv_max_f32_dpp %0, %0, %0 row_ror:2 row_mask:0xf bank_mask:0xf\n\t"
                 "s_nop 1\n\tv_max_f32_dpp %0, %0, %0 row_ror:4 row_mask:0xf bank_mask:0xf\n\ts_nop 1\n\tv_max_f32_dpp %0, %0, %0 row_ror:8 row_mask:0xf bank_mask:0xf\n\ts_nop 0" : "+v"(v));
    return v;
}
DI float max64(float v) { v = max16(v); v = fmaxf(v, xor16(v)); return fmaxf(v, xor32(v)); }
DI int swap23(int i) { return (i & ~12) | ((i & 4) << 1) | ((i & 8) >> 1); }

#define ATTN_LOOP_ASM \
    "ds_read_b128 v[88:91], v112 offset:0\n\t" \
    "ds_read_b128 v[96:99], v113 offset:0\n\t" \
    "ds_read_b128 v[100:103], v113 offset:512\n\t" \
    "s_waitcnt lgkmcnt(0)\n\t" \
    "v_mfma_f32_32x32x16_bf16 v[48:63], v[88:91], v[80:83], 0\n\t" \
    "s_mov_b32 %[cnt], 13\n\t" \
    "s_nop 7\n\t" \
    "s_nop 2\n\t" \
    "v_exp_f32 v48, v48\n\t" \
    "s_cmp_eq_u32 %[ldr], 0\n\t" \
    "s_cbranch_scc1 Lga0%=\n\t" \
    "s_mov_b32 m0, s64\n\t" \
    "s_add_u32 s64, s64, 0x2000\n\t" \
    "global_load_lds_dwordx4 %[gvo], s[60:61] sc1\n\t" \
    "global_load_lds_dwordx4 %[gvo], s[60:61] offset:1024 sc1\n\t" \
    "s_add_u32 s60, s60, 0x2000\n\t" \
    "s_addc_u32 s61, s61, 0\n\t" \
    "s_mov_b32 m0, s64\n\t" \
    "s_add_u32 s64, s64, 0x2000\n\t" \
    "global_load_lds_dwordx4 %[gvo], s[60:61] sc1\n\t" \
    "global_load_lds_dwordx4 %[gvo], s[60:61] offset:1024 sc1\n\t" \
    "s_add_u32 s60, s60, 0x2000\n\t" \
    "s_addc_u32 s61, s61, 0\n\t" \
    "Lga0%=:\n\t" \
    "v_exp_f32 v49, v49\n\t" \
    "v_exp_f32 v50, v50\n\t" \
    "v_exp_f32 v51, v51\n\t" \
    "ds_read_b128 v[92:95], v112 offset:2048\n\t" \
    "v_exp_f32 v52, v52\n\t" \
    "v_exp_f32 v53, v53\n\t" \
    "ds_read_b128 v[104:107], v113 offset:2048\n\t" \
    "v_exp_f32 v54, v54\n\t" \
    "v_mfma_f32_32x32x16_bf16 v[64:79], v[88:91], v[84:87], 0\n\t" \
    "v_exp_f32 v55, v55\n\t" \
    "v_exp_f32 v56, v56\n\t" \
    "ds_read_b128 v[108:111], v113 offset:2560\n\t" \
    "v_exp_f32 v57, v57\n\t" \
    "v_exp_f32 v58, v58\n\t" \
    "v_exp_f32 v59, v59\n\t" \
    "v_smfmac_f32_32x32x32_bf16 v[16:31], v[96:99], v[48:55], v115\n\t" \
    "v_exp_f32 v60, v60\n\t" \
    "v_exp_f32 v61, v61\n\t" \
    "v_exp_f32 v62, v62\n\t" \
    "v_exp_f32 v63, v63\n\t" \
    "v_exp_f32 v64, v64\n\t" \
    "v_exp_f32 v65, v65\n\t" \
    "v_exp_f32 v66, v66\n\t" \
    "v_smfmac_f32_32x32x32_bf16 v[16:31], v[100:103], v[56:63], v115\n\t" \
    "v_exp_f32 v67, v67\n\t" \
    "v_exp_f32 v68, v68\n\t" \
    "v_exp_f32 v69, v69\n\t" \
    "v_exp_f32 v70, v70\n\t" \
    "s_waitcnt lgkmcnt(0)\n\t" \
    "v_mfma_f32_32x32x16_bf16 v[48:63], v[92:95], v[80:83], 0\n\t" \
    "v_exp_f32 v71, v71\n\t" \
    "v_exp_f32 v72, v72\n\t" \
    "v_exp_f32 v73, v73\n\t" \
    "v_exp_f32 v74, v74\n\t" \
    "v_exp_f32 v75, v75\n\t" \
    "v_smfmac_f32_32x32x32_bf16 v[32:47], v[96:99], v[64:71], v115\n\t" \
    "v_exp_f32 v76, v76\n\t" \
    "v_exp_f32 v77, v77\n\t" \
    "v_exp_f32 v78, v78\n\t" \
    "v_exp_f32 v79, v79\n\t" \
    "v_exp_f32 v48, v48\n\t" \
    "s_cmp_eq_u32 %[ldr], 0\n\t" \
    "s_cbranch_scc1 Lga1%=\n\t" \
    "s_mov_b32 m0, s64\n\t" \
    "s_add_u32 s64, s64, 0x2000\n\t" \
    "global_load_lds_dwordx4 %[gvo], s[60:61] sc1\n\t" \
    "global_load_lds_dwordx4 %[gvo], s[60:61] offset:1024 sc1\n\t" \
    "s_add_u32 s60, s60, 0x2000\n\t" \
    "s_addc_u32 s61, s61, 0\n\t" \
    "s_mov_b32 m0, s64\n\t" \
    "s_add_u32 s64, s64, 0x2000\n\t" \
    "global_load_lds_dwordx4 %[gvo], s[60:61] sc1\n\t" \
    "global_load_lds_dwordx4 %[gvo], s[60:61] offset:1024 sc1\n\t" \
    "s_add_u32 s60, s60, 0x2000\n\t" \
    "s_addc_u32 s61, s61, 0\n\t" \
    "Lga1%=:\n\t" \
    "v_exp_f32 v49, v49\n\t" \
    "v_exp_f32 v50, v50\n\t" \
    "v_smfmac_f32_32x32x32_bf16 v[32:47], v[100:103], v[72:79], v115\n\t" \
    "v_exp_f32 v51, v51\n\t" \
    "ds_read_b128 v[88:91], v112 offset:4096\n\t" \
    "v_exp_f32 v52, v52\n\t" \
    "v_exp_f32 v53, v53\n\t" \
    "ds_read_b128 v[96:99], v113 offset:4096\n\t" \
    "v_exp_f32 v54, v54\n\t" \
    "v_mfma_f32_32x32x16_bf16 v[64:79], v[92:95], v[84:87], 0\n\t" \
    "v_exp_f32 v55, v55\n\t" \
    "v_exp_f32 v56, v56\n\t" \
    "ds_read_b128 v[100:103], v113 offset:4608\n\t" \
    "v_exp_f32 v57, v57\n\t" \
    "v_exp_f32 v58, v58\n\t" \
    "v_exp_f32 v59, v59\n\t" \
    "v_smfmac_f32_32x32x32_bf16 v[16:31], v[104:107], v[48:55], v115\n\t" \
    "v_exp_f32 v60, v60\n\t" \
    "v_exp_f32 v61, v61\n\t" \
    "v_exp_f32 v62, v62\n\t" \
    "v_exp_f32 v63, v63\n\t" \
    "v_exp_f32 v64, v64\n\t" \
    "v_exp_f32 v65, v65\n\t" \
    "v_exp_f32 v66, v66\n\t" \
    "v_smfmac_f32_32x32x32_bf16 v[16:31], v[108:111], v[56:63], v115\n\t" \
    "v_exp_f32 v67, v67\n\t" \
    "v_exp_f32 v68, v68\n\t" \
    "v_exp_f32 v69, v69\n\t" \
    "v_exp_f32 v70, v70\n\t" \
    "s_waitcnt lgkmcnt(0)\n\t" \
    "v_mfma_f32_32x32x16_bf16 v[48:63], v[88:91], v[80:83], 0\n\t" \
    "v_exp_f32 v71, v71\n\t" \
    "v_exp_f32 v72, v72\n\t" \
    "v_exp_f32 v73, v73\n\t" \
    "v_exp_f32 v74, v74\n\t" \
    "v_exp_f32 v75, v75\n\t" \
    "v_smfmac_f32_32x32x32_bf16 v[32:47], v[104:107], v[64:71], v115\n\t" \
    "v_exp_f32 v76, v76\n\t" \
    "v_exp_f32 v77, v77\n\t" \
    "v_exp_f32 v78, v78\n\t" \
    "v_exp_f32 v79, v79\n\t" \
    "v_exp_f32 v48, v48\n\t" \
    "s_cmp_eq_u32 %[ldr], 0\n\t" \
    "s_cbranch_scc1 Lga2%=\n\t" \
    "s_mov_b32 m0, s64\n\t" \
    "s_add_u32 s64, s64, 0x2000\n\t" \
    "global_load_lds_dwordx4 %[gvo], s[60:61] sc1\n\t" \
    "global_load_lds_dwordx4 %[gvo], s[60:61] offset:1024 sc1\n\t" \
    "s_add_u32 s60, s60, 0x2000\n\t" \
    "s_addc_u32 s61, s61, 0\n\t" \
    "s_mov_b32 m0, s64\n\t" \
    "s_add_u32 s64, s64, 0x2000\n\t" \
    "global_load_lds_dwordx4 %[gvo], s[60:61] sc1\n\t" \
    "global_load_lds_dwordx4 %[gvo], s[60:61] offset:1024 sc1\n\t" \
    "s_add_u32 s60, s60, 0x2000\n\t" \
    "s_addc_u32 s61, s61, 0\n\t" \
    "Lga2%=:\n\t" \
    "v_exp_f32 v49, v49\n\t" \
    "v_exp_f32 v50, v50\n\t" \
    "v_smfmac_f32_32x32x32_bf16 v[32:47], v[108:111], v[72:79], v115\n\t" \
    "v_exp_f32 v51, v51\n\t" \
    "ds_read_b128 v[92:95], v112 offset:6144\n\t" \
    "v_exp_f32 v52, v52\n\t" \
    "v_exp_f32 v53, v53\n\t" \
    "ds_read_b128 v[104:107], v113 offset:6144\n\t" \
    "v_exp_f32 v54, v54\n\t" \
    "v_mfma_f32_32x32x16_bf16 v[64:79], v[88:91], v[84:87], 0\n\t" \
    "v_exp_f32 v55, v55\n\t" \
    "v_exp_f32 v56, v56\n\t" \
    "ds_read_b128 v[108:111], v113 offset:6656\n\t" \
    "v_exp_f32 v57, v57\n\t" \
    "v_exp_f32 v58, v58\n\t" \
    "v_exp_f32 v59, v59\n\t" \
    "v_smfmac_f32_32x32x32_bf16 v[16:31], v[96:99], v[48:55], v115\n\t" \
    "v_exp_f32 v60, v60\n\t" \
    "v_exp_f32 v61, v61\n\t" \
    "v_exp_f32 v62, v62\n\t" \
    "v_exp_f32 v63, v63\n\t" \
    "v_exp_f32 v64, v64\n\t" \
    "v_exp_f32 v65, v65\n\t" \
    "v_exp_f32 v66, v66\n\t" \
    "v_smfmac_f32_32x32x32_bf16 v[16:31], v[100:103], v[56:63], v115\n\t" \
    "v_exp_f32 v67, v67\n\t" \
    "v_exp_f32 v68, v68\n\t" \
    "v_exp_f32 v69, v69\n\t" \
    "v_exp_f32 v70, v70\n\t" \
    "s_waitcnt lgkmcnt(0)\n\t" \
    "v_mfma_f32_32x32x16_bf16 v[48:63], v[92:95], v[80:83], 0\n\t" \
    "v_exp_f32 v71, v71\n\t" \
    "v_exp_f32 v72, v72\n\t" \
    "v_exp_f32 v73, v73\n\t" \
    "v_exp_f32 v74, v74\n\t" \
    "v_exp_f32 v75, v75\n\t" \
    "v_smfmac_f32_32x32x32_bf16 v[32:47], v[96:99], v[64:71], v115\n\t" \
    "v_exp_f32 v76, v76\n\t" \
    "v_exp_f32 v77, v77\n\t" \
    "v_exp_f32 v78, v78\n\t" \
    "v_exp_f32 v79, v79\n\t" \
    "v_exp_f32 v48, v48\n\t" \
    "s_cmp_eq_u32 %[ldr], 0\n\t" \
    "s_cbranch_scc1 Lga3%=\n\t" \
    "s_mov_b32 m0, s64\n\t" \
    "s_add_u32 s64, s64, 0x2000\n\t" \
    "global_load_lds_dwordx4 %[gvo], s[60:61] sc1\n\t" \
    "global_load_lds_dwordx4 %[gvo], s[60:61] offset:1024 sc1\n\t" \
    "s_add_u32 s60, s60, 0x2000\n\t" \
    "s_addc_u32 s61, s61, 0\n\t" \
    "s_mov_b32 m0, s64\n\t" \
    "s_add_u32 s64, s64, 0x2000\n\t" \
    "global_load_lds_dwordx4 %[gvo], s[60:61] sc1\n\t" \
    "global_load_lds_dwordx4 %[gvo], s[60:61] offset:1024 sc1\n\t" \
    "s_add_u32 s60, s60, 0x2000\n\t" \
    "s_addc_u32 s61, s61, 0\n\t" \
    "Lga3%=:\n\t" \
    "v_exp_f32 v49, v49\n\t" \
    "v_exp_f32 v50, v50\n\t" \
    "v_smfmac_f32_32x32x32_bf16 v[32:47], v[100:103], v[72:79], v115\n\t" \
    "v_exp_f32 v51, v51\n\t" \
    "ds_read_b128 v[88:91], v112 offset:8192\n\t" \
    "v_exp_f32 v52, v52\n\t" \
    "v_exp_f32 v53, v53\n\t" \
    "ds_read_b128 v[96:99], v113 offset:8192\n\t" \
    "v_exp_f32 v54, v54\n\t" \
    "v_mfma_f32_32x32x16_bf16 v[64:79], v[92:95], v[84:87], 0\n\t" \
    "v_exp_f32 v55, v55\n\t" \
    "v_exp_f32 v56, v56\n\t" \
    "ds_read_b128 v[100:103], v113 offset:8704\n\t" \
    "v_exp_f32 v57, v57\n\t" \
    "v_exp_f32 v58, v58\n\t" \
    "v_exp_f32 v59, v59\n\t" \
    "v_smfmac_f32_32x32x32_bf16 v[16:31], v[104:107], v[48:55], v115\n\t" \
    "v_exp_f32 v60, v60\n\t" \
    "v_exp_f32 v61, v61\n\t" \
    "v_exp_f32 v62, v62\n\t" \
    "v_exp_f32 v63, v63\n\t" \
    "v_exp_f32 v64, v64\n\t" \
    "v_exp_f32 v65, v65\n\t" \
    "v_exp_f32 v66, v66\n\t" \
    "v_smfmac_f32_32x32x32_bf16 v[16:31], v[108:111], v[56:63], v115\n\t" \
    "v_exp_f32 v67, v67\n\t" \
    "v_exp_f32 v68, v68\n\t" \
    "v_exp_f32 v69, v69\n\t" \
    "v_exp_f32 v70, v70\n\t" \
    "s_waitcnt lgkmcnt(0)\n\t" \
    "v_mfma_f32_32x32x16_bf16 v[48:63], v[88:91], v[80:83], 0\n\t" \
    "v_exp_f32 v71, v71\n\t" \
    "v_exp_f32 v72, v72\n\t" \
    "v_exp_f32 v73, v73\n\t" \
    "v_exp_f32 v74, v74\n\t" \
    "v_exp_f32 v75, v75\n\t" \
    "v_smfmac_f32_32x32x32_bf16 v[32:47], v[104:107], v[64:71], v115\n\t" \
    "v_exp_f32 v76, v76\n\t" \
    "v_exp_f32 v77, v77\n\t" \
    "v_add_u32 v112, 8192, v112\n\t" \
    "v_add_u32 v113, v114, v113\n\t" \
    "v_exp_f32 v78, v78\n\t" \
    "v_exp_f32 v79, v79\n\t" \
    "v_exp_f32 v48, v48\n\t" \
    "s_cmp_eq_u32 %[ldr], 0\n\t" \
    "s_cbranch_scc1 Lgb0%=\n\t" \
    "s_mov_b32 m0, s64\n\t" \
    "s_add_u32 s64, s64, 0x2000\n\t" \
    "global_load_lds_dwordx4 %[gvo], s[60:61] sc1\n\t" \
    "global_load_lds_dwordx4 %[gvo], s[60:61] offset:1024 sc1\n\t" \
    "s_add_u32 s60, s60, 0x2000\n\t" \
    "s_addc_u32 s61, s61, 0\n\t" \
    "s_mov_b32 m0, s64\n\t" \
    "s_add_u32 s64, s64, 0x2000\n\t" \
    "global_load_lds_dwordx4 %[gvo], s[60:61] sc1\n\t" \
    "global_load_lds_dwordx4 %[gvo], s[60:61] offset:1024 sc1\n\t" \
    "s_add_u32 s60, s60, 0x2000\n\t" \
    "s_addc_u32 s61, s61, 0\n\t" \
    "Lgb0%=:\n\t" \
    "v_exp_f32 v49, v49\n\t" \
    "v_exp_f32 v50, v50\n\t" \
    "v_smfmac_f32_32x32x32_bf16 v[32:47], v[108:111], v[72:79], v115\n\t" \
    "v_exp_f32 v51, v51\n\t" \
    "ds_read_b128 v[92:95], v112 offset:2048\n\t" \
    "v_exp_f32 v52, v52\n\t" \
    "v_exp_f32 v53, v53\n\t" \
    "ds_read_b128 v[104:107], v113 offset:2048\n\t" \
    "v_exp_f32 v54, v54\n\t" \
    "v_mfma_f32_32x32x16_bf16 v[64:79], v[88:91], v[84:87], 0\n\t" \
    "v_exp_f32 v55, v55\n\t" \
    "v_exp_f32 v56, v56\n\t" \
    "ds_read_b128 v[108:111], v113 offset:2560\n\t" \
    "v_exp_f32 v57, v57\n\t" \
    "v_exp_f32 v58, v58\n\t" \
    "v_exp_f32 v59, v59\n\t" \
    "v_smfmac_f32_32x32x32_bf16 v[16:31], v[96:99], v[48:55], v115\n\t" \
    "v_exp_f32 v60, v60\n\t" \
    "v_exp_f32 v61, v61\n\t" \
    "v_exp_f32 v62, v62\n\t" \
    "v_exp_f32 v63, v63\n\t" \
    "v_exp_f32 v64, v64\n\t" \
    "v_exp_f32 v65, v65\n\t" \
    "v_exp_f32 v66, v66\n\t" \
    "v_smfmac_f32_32x32x32_bf16 v[16:31], v[100:103], v[56:63], v115\n\t" \
    "v_exp_f32 v67, v67\n\t" \
    "v_exp_f32 v68, v68\n\t" \
    "v_exp_f32 v69, v69\n\t" \
    "v_exp_f32 v70, v70\n\t" \
    "s_waitcnt lgkmcnt(0)\n\t" \
    "v_mfma_f32_32x32x16_bf16 v[48:63], v[92:95], v[80:83], 0\n\t" \
    "v_exp_f32 v71, v71\n\t" \
    "v_exp_f32 v72, v72\n\t" \
    "v_exp_f32 v73, v73\n\t" \
    "v_exp_f32 v74, v74\n\t" \
    "v_exp_f32 v75, v75\n\t" \
    "v_smfmac_f32_32x32x32_bf16 v[32:47], v[96:99], v[64:71], v115\n\t" \
    "v_exp_f32 v76, v76\n\t" \
    "v_exp_f32 v77, v77\n\t" \
    "v_exp_f32 v78, v78\n\t" \
    "v_exp_f32 v79, v79\n\t" \
    "v_exp_f32 v48, v48\n\t" \
    "s_cmp_eq_u32 %[ldr], 0\n\t" \
    "s_cbranch_scc1 Lgb1%=\n\t" \
    "s_mov_b32 m0, s64\n\t" \
    "s_add_u32 s64, s64, 0x2000\n\t" \
    "global_load_lds_dwordx4 %[gvo], s[60:61] sc1\n\t" \
    "global_load_lds_dwordx4 %[gvo], s[60:61] offset:1024 sc1\n\t" \
    "s_add_u32 s60, s60, 0x2000\n\t" \
    "s_addc_u32 s61, s61, 0\n\t" \
    "s_mov_b32 m0, s64\n\t" \
    "s_add_u32 s64, s64, 0x2000\n\t" \
    "global_load_lds_dwordx4 %[gvo], s[60:61] sc1\n\t" \
    "global_load_lds_dwordx4 %[gvo], s[60:61] offset:1024 sc1\n\t" \
    "s_add_u32 s60, s60, 0x2000\n\t" \
    "s_addc_u32 s61, s61, 0\n\t" \
    "Lgb1%=:\n\t" \
    "v_exp_f32 v49, v49\n\t" \
    "v_exp_f32 v50, v50\n\t" \
    "v_smfmac_f32_32x32x32_bf16 v[32:47], v[100:103], v[72:79], v115\n\t" \
    "v_exp_f32 v51, v51\n\t" \
    "ds_read_b128 v[88:91], v112 offset:4096\n\t" \
    "v_exp_f32 v52, v52\n\t" \
    "v_exp_f32 v53, v53\n\t" \
    "ds_read_b128 v[96:99], v113 offset:4096\n\t" \
    "v_exp_f32 v54, v54\n\t" \
    "v_mfma_f32_32x32x16_bf16 v[64:79], v[92:95], v[84:87], 0\n\t" \
    "v_exp_f32 v55, v55\n\t" \
    "v_exp_f32 v56, v56\n\t" \
    "ds_read_b128 v[100:103], v113 offset:4608\n\t" \
    "v_exp_f32 v57, v57\n\t" \
    "v_exp_f32 v58, v58\n\t" \
    "v_exp_f32 v59, v59\n\t" \
    "v_smfmac_f32_32x32x32_bf16 v[16:31], v[104:107], v[48:55], v115\n\t" \
    "v_exp_f32 v60, v60\n\t" \
    "v_exp_f32 v61, v61\n\t" \
    "v_exp_f32 v62, v62\n\t" \
    "v_exp_f32 v63, v63\n\t" \
    "v_exp_f32 v64, v64\n\t" \
    "v_exp_f32 v65, v65\n\t" \
    "v_exp_f32 v66, v66\n\t" \
    "v_smfmac_f32_32x32x32_bf16 v[16:31], v[108:111], v[56:63], v115\n\t" \
    "v_exp_f32 v67, v67\n\t" \
    "v_exp_f32 v68, v68\n\t" \
    "v_exp_f32 v69, v69\n\t" \
    "v_exp_f32 v70, v70\n\t" \
    "s_waitcnt lgkmcnt(0)\n\t" \
    "v_mfma_f32_32x32x16_bf16 v[48:63], v[88:91], v[80:83], 0\n\t" \
    "v_exp_f32 v71, v71\n\t" \
    "v_exp_f32 v72, v72\n\t" \
    "v_exp_f32 v73, v73\n\t" \
    "v_exp_f32 v74, v74\n\t" \
    "v_exp_f32 v75, v75\n\t" \
    "v_smfmac_f32_32x32x32_bf16 v[32:47], v[104:107], v[64:71], v115\n\t" \
    "v_exp_f32 v76, v76\n\t" \
    "v_exp_f32 v77, v77\n\t" \
    "v_exp_f32 v78, v78\n\t" \
    "v_exp_f32 v79, v79\n\t" \
    "v_exp_f32 v48, v48\n\t" \
    "s_cmp_eq_u32 %[ldr], 0\n\t" \
    "s_cbranch_scc1 Lgb2%=\n\t" \
    "s_mov_b32 m0, s64\n\t" \
    "s_add_u32 s64, s64, 0x2000\n\t" \
    "global_load_lds_dwordx4 %[gvo], s[60:61] sc1\n\t" \
    "global_load_lds_dwordx4 %[gvo], s[60:61] offset:1024 sc1\n\t" \
    "s_add_u32 s60, s60, 0x2000\n\t" \
    "s_addc_u32 s61, s61, 0\n\t" \
    "Lgb2%=:\n\t" \
    "v_exp_f32 v49, v49\n\t" \
    "v_exp_f32 v50, v50\n\t" \
    "v_smfmac_f32_32x32x32_bf16 v[32:47], v[108:111], v[72:79], v115\n\t" \
    "v_exp_f32 v51, v51\n\t" \
    "ds_read_b128 v[92:95], v112 offset:6144\n\t" \
    "v_exp_f32 v52, v52\n\t" \
    "v_exp_f32 v53, v53\n\t" \
    "ds_read_b128 v[104:107], v113 offset:6144\n\t" \
    "v_exp_f32 v54, v54\n\t" \
    "v_mfma_f32_32x32x16_bf16 v[64:79], v[88:91], v[84:87], 0\n\t" \
    "v_exp_f32 v55, v55\n\t" \
    "v_exp_f32 v56, v56\n\t" \
    "ds_read_b128 v[108:111], v113 offset:6656\n\t" \
    "v_exp_f32 v57, v57\n\t" \
    "v_exp_f32 v58, v58\n\t" \
    "v_exp_f32 v59, v59\n\t" \
    "v_smfmac_f32_32x32x32_bf16 v[16:31], v[96:99], v[48:55], v115\n\t" \
    "v_exp_f32 v60, v60\n\t" \
    "v_exp_f32 v61, v61\n\t" \
    "v_exp_f32 v62, v62\n\t" \
    "v_exp_f32 v63, v63\n\t" \
    "v_exp_f32 v64, v64\n\t" \
    "v_exp_f32 v65, v65\n\t" \
    "v_exp_f32 v66, v66\n\t" \
    "v_smfmac_f32_32x32x32_bf16 v[16:31], v[100:103], v[56:63], v115\n\t" \
    "v_exp_f32 v67, v67\n\t" \
    "v_exp_f32 v68, v68\n\t" \
    "v_exp_f32 v69, v69\n\t" \
    "v_exp_f32 v70, v70\n\t" \
    "s_waitcnt lgkmcnt(0)\n\t" \
    "v_mfma_f32_32x32x16_bf16 v[48:63], v[92:95], v[80:83], 0\n\t" \
    "v_exp_f32 v71, v71\n\t" \
    "v_exp_f32 v72, v72\n\t" \
    "v_exp_f32 v73, v73\n\t" \
    "v_exp_f32 v74, v74\n\t" \
    "v_exp_f32 v75, v75\n\t" \
    "v_smfmac_f32_32x32x32_bf16 v[32:47], v[96:99], v[64:71], v115\n\t" \
    "v_exp_f32 v76, v76\n\t" \
    "v_exp_f32 v77, v77\n\t" \
    "v_exp_f32 v78, v78\n\t" \
    "v_exp_f32 v79, v79\n\t" \
    "v_exp_f32 v48, v48\n\t" \
    "v_exp_f32 v49, v49\n\t" \
    "v_exp_f32 v50, v50\n\t" \
    "v_smfmac_f32_32x32x32_bf16 v[32:47], v[100:103], v[72:79], v115\n\t" \
    "v_exp_f32 v51, v51\n\t" \
    "ds_read_b128 v[88:91], v112 offset:8192\n\t" \
    "v_exp_f32 v52, v52\n\t" \
    "v_exp_f32 v53, v53\n\t" \
    "ds_read_b128 v[96:99], v113 offset:8192\n\t" \
    "v_exp_f32 v54, v54\n\t" \
    "v_mfma_f32_32x32x16_bf16 v[64:79], v[92:95], v[84:87], 0\n\t" \
    "v_exp_f32 v55, v55\n\t" \
    "v_exp_f32 v56, v56\n\t" \
    "ds_read_b128 v[100:103], v113 offset:8704\n\t" \
    "v_exp_f32 v57, v57\n\t" \
    "v_exp_f32 v58, v58\n\t" \
    "v_exp_f32 v59, v59\n\t" \
    "v_smfmac_f32_32x32x32_bf16 v[16:31], v[104:107], v[48:55], v115\n\t" \
    "v_exp_f32 v60, v60\n\t" \
    "v_exp_f32 v61, v61\n\t" \
    "v_exp_f32 v62, v62\n\t" \
    "v_exp_f32 v63, v63\n\t" \
    "v_exp_f32 v64, v64\n\t" \
    "v_exp_f32 v65, v65\n\t" \
    "v_exp_f32 v66, v66\n\t" \
    "v_smfmac_f32_32x32x32_bf16 v[16:31], v[108:111], v[56:63], v115\n\t" \
    "v_exp_f32 v67, v67\n\t" \
    "v_exp_f32 v68, v68\n\t" \
    "v_exp_f32 v69, v69\n\t" \
    "v_exp_f32 v70, v70\n\t" \
    "s_waitcnt lgkmcnt(0)\n\t" \
    "v_mfma_f32_32x32x16_bf16 v[48:63], v[88:91], v[80:83], 0\n\t" \
    "v_exp_f32 v71, v71\n\t" \
    "v_exp_f32 v72, v72\n\t" \
    "v_exp_f32 v73, v73\n\t" \
    "v_exp_f32 v74, v74\n\t" \
    "v_exp_f32 v75, v75\n\t" \
    "v_smfmac_f32_32x32x32_bf16 v[32:47], v[104:107], v[64:71], v115\n\t" \
    "v_exp_f32 v76, v76\n\t" \
    "v_exp_f32 v77, v77\n\t" \
    "v_add_u32 v112, 8192, v112\n\t" \
    "v_add_u32 v113, v114, v113\n\t" \
    "v_exp_f32 v78, v78\n\t" \
    "v_exp_f32 v79, v79\n\t" \
    "v_exp_f32 v48, v48\n\t" \
    "v_exp_f32 v49, v49\n\t" \
    "v_exp_f32 v50, v50\n\t" \
    "v_smfmac_f32_32x32x32_bf16 v[32:47], v[108:111], v[72:79], v115\n\t" \
    "v_exp_f32 v51, v51\n\t" \
    "ds_read_b128 v[92:95], v112 offset:2048\n\t" \
    "v_exp_f32 v52, v52\n\t" \
    "v_exp_f32 v53, v53\n\t" \
    "ds_read_b128 v[104:107], v113 offset:2048\n\t" \
    "v_exp_f32 v54, v54\n\t" \
    "v_mfma_f32_32x32x16_bf16 v[64:79], v[88:91], v[84:87], 0\n\t" \
    "v_exp_f32 v55, v55\n\t" \
    "v_exp_f32 v56, v56\n\t" \
    "ds_read_b128 v[108:111], v113 offset:2560\n\t" \
    "v_exp_f32 v57, v57\n\t" \
    "v_exp_f32 v58, v58\n\t" \
    "v_exp_f32 v59, v59\n\t" \
    "v_smfmac_f32_32x32x32_bf16 v[16:31], v[96:99], v[48:55], v115\n\t" \
    "v_exp_f32 v60, v60\n\t" \
    "v_exp_f32 v61, v61\n\t" \
    "v_exp_f32 v62, v62\n\t" \
    "v_exp_f32 v63, v63\n\t" \
    "v_exp_f32 v64, v64\n\t" \
    "v_exp_f32 v65, v65\n\t" \
    "v_exp_f32 v66, v66\n\t" \
    "v_smfmac_f32_32x32x32_bf16 v[16:31], v[100:103], v[56:63], v115\n\t" \
    "v_exp_f32 v67, v67\n\t" \
    "v_exp_f32 v68, v68\n\t" \
    "v_exp_f32 v69, v69\n\t" \
    "v_exp_f32 v70, v70\n\t" \
    "s_waitcnt lgkmcnt(0)\n\t" \
    "v_mfma_f32_32x32x16_bf16 v[48:63], v[92:95], v[80:83], 0\n\t" \
    "v_exp_f32 v71, v71\n\t" \
    "v_exp_f32 v72, v72\n\t" \
    "v_exp_f32 v73, v73\n\t" \
    "v_exp_f32 v74, v74\n\t" \
    "v_exp_f32 v75, v75\n\t" \
    "v_smfmac_f32_32x32x32_bf16 v[32:47], v[96:99], v[64:71], v115\n\t" \
    "v_exp_f32 v76, v76\n\t" \
    "v_exp_f32 v77, v77\n\t" \
    "v_exp_f32 v78, v78\n\t" \
    "v_exp_f32 v79, v79\n\t" \
    "v_exp_f32 v48, v48\n\t" \
    "v_exp_f32 v49, v49\n\t" \
    "v_exp_f32 v50, v50\n\t" \
    "v_smfmac_f32_32x32x32_bf16 v[32:47], v[100:103], v[72:79], v115\n\t" \
    "v_exp_f32 v51, v51\n\t" \
    "ds_read_b128 v[88:91], v112 offset:4096\n\t" \
    "v_exp_f32 v52, v52\n\t" \
    "v_exp_f32 v53, v53\n\t" \
    "ds_read_b128 v[96:99], v113 offset:4096\n\t" \
    "v_exp_f32 v54, v54\n\t" \
    "v_mfma_f32_32x32x16_bf16 v[64:79], v[92:95], v[84:87], 0\n\t" \
    "v_exp_f32 v55, v55\n\t" \
    "v_exp_f32 v56, v56\n\t" \
    "ds_read_b128 v[100:103], v113 offset:4608\n\t" \
    "v_exp_f32 v57, v57\n\t" \
    "v_exp_f32 v58, v58\n\t" \
    "v_exp_f32 v59, v59\n\t" \
    "v_smfmac_f32_32x32x32_bf16 v[16:31], v[104:107], v[48:55], v115\n\t" \
    "v_exp_f32 v60, v60\n\t" \
    "v_exp_f32 v61, v61\n\t" \
    "v_exp_f32 v62, v62\n\t" \
    "v_exp_f32 v63, v63\n\t" \
    "v_exp_f32 v64, v64\n\t" \
    "v_exp_f32 v65, v65\n\t" \
    "v_exp_f32 v66, v66\n\t" \
    "v_smfmac_f32_32x32x32_bf16 v[16:31], v[108:111], v[56:63], v115\n\t" \
    "v_exp_f32 v67, v67\n\t" \
    "v_exp_f32 v68, v68\n\t" \
    "v_exp_f32 v69, v69\n\t" \
    "v_exp_f32 v70, v70\n\t" \
    "s_waitcnt lgkmcnt(0)\n\t" \
    "v_mfma_f32_32x32x16_bf16 v[48:63], v[88:91], v[80:83], 0\n\t" \
    "v_exp_f32 v71, v71\n\t" \
    "v_exp_f32 v72, v72\n\t" \
    "v_exp_f32 v73, v73\n\t" \
    "v_exp_f32 v74, v74\n\t" \
    "v_exp_f32 v75, v75\n\t" \
    "v_smfmac_f32_32x32x32_bf16 v[32:47], v[104:107], v[64:71], v115\n\t" \
    "v_exp_f32 v76, v76\n\t" \
    "v_exp_f32 v77, v77\n\t" \
    "v_exp_f32 v78, v78\n\t" \
    "v_exp_f32 v79, v79\n\t" \
    "v_exp_f32 v48, v48\n\t" \
    "v_exp_f32 v49, v49\n\t" \
    "v_exp_f32 v50, v50\n\t" \
    "v_smfmac_f32_32x32x32_bf16 v[32:47], v[108:111], v[72:79], v115\n\t" \
    "v_exp_f32 v51, v51\n\t" \
    "ds_read_b128 v[92:95], v112 offset:6144\n\t" \
    "v_exp_f32 v52, v52\n\t" \
    "v_exp_f32 v53, v53\n\t" \
    "ds_read_b128 v[104:107], v113 offset:6144\n\t" \
    "v_exp_f32 v54, v54\n\t" \
    "v_mfma_f32_32x32x16_bf16 v[64:79], v[88:91], v[84:87], 0\n\t" \
    "v_exp_f32 v55, v55\n\t" \
    "v_exp_f32 v56, v56\n\t" \
    "ds_read_b128 v[108:111], v113 offset:6656\n\t" \
    "v_exp_f32 v57, v57\n\t" \
    "v_exp_f32 v58, v58\n\t" \
    "v_exp_f32 v59, v59\n\t" \
    "v_smfmac_f32_32x32x32_bf16 v[16:31], v[96:99], v[48:55], v115\n\t" \
    "v_exp_f32 v60, v60\n\t" \
    "v_exp_f32 v61, v61\n\t" \
    "v_exp_f32 v62, v62\n\t" \
    "v_exp_f32 v63, v63\n\t" \
    "v_exp_f32 v64, v64\n\t" \
    "v_exp_f32 v65, v65\n\t" \
    "v_exp_f32 v66, v66\n\t" \
    "v_smfmac_f32_32x32x32_bf16 v[16:31], v[100:103], v[56:63], v115\n\t" \
    "v_exp_f32 v67, v67\n\t" \
    "v_exp_f32 v68, v68\n\t" \
    "v_exp_f32 v69, v69\n\t" \
    "v_exp_f32 v70, v70\n\t" \
    "s_waitcnt lgkmcnt(0)\n\t" \
    "v_mfma_f32_32x32x16_bf16 v[48:63], v[92:95], v[80:83], 0\n\t" \
    "v_exp_f32 v71, v71\n\t" \
    "v_exp_f32 v72, v72\n\t" \
    "v_exp_f32 v73, v73\n\t" \
    "v_exp_f32 v74, v74\n\t" \
    "v_exp_f32 v75, v75\n\t" \
    "v_smfmac_f32_32x32x32_bf16 v[32:47], v[96:99], v[64:71], v115\n\t" \
    "v_exp_f32 v76, v76\n\t" \
    "v_exp_f32 v77, v77\n\t" \
    "v_exp_f32 v78, v78\n\t" \
    "v_exp_f32 v79, v79\n\t" \
    "s_waitcnt vmcnt(0)\n\t" \
    "s_barrier\n\t" \
    "v_exp_f32 v48, v48\n\t" \
    "v_exp_f32 v49, v49\n\t" \
    "v_exp_f32 v50, v50\n\t" \
    "v_smfmac_f32_32x32x32_bf16 v[32:47], v[100:103], v[72:79], v115\n\t" \
    "v_exp_f32 v51, v51\n\t" \
    "ds_read_b128 v[88:91], v112 offset:8192\n\t" \
    "v_exp_f32 v52, v52\n\t" \
    "v_exp_f32 v53, v53\n\t" \
    "ds_read_b128 v[96:99], v113 offset:8192\n\t" \
    "v_exp_f32 v54, v54\n\t" \
    "v_mfma_f32_32x32x16_bf16 v[64:79], v[92:95], v[84:87], 0\n\t" \
    "v_exp_f32 v55, v55\n\t" \
    "v_exp_f32 v56, v56\n\t" \
    "ds_read_b128 v[100:103], v113 offset:8704\n\t" \
    "v_exp_f32 v57, v57\n\t" \
    "v_exp_f32 v58, v58\n\t" \
    "v_exp_f32 v59, v59\n\t" \
    "v_smfmac_f32_32x32x32_bf16 v[16:31], v[104:107], v[48:55], v115\n\t" \
    "v_exp_f32 v60, v60\n\t" \
    "v_exp_f32 v61, v61\n\t" \
    "v_exp_f32 v62, v62\n\t" \
    "v_exp_f32 v63, v63\n\t" \
    "v_exp_f32 v64, v64\n\t" \
    "v_exp_f32 v65, v65\n\t" \
    "v_exp_f32 v66, v66\n\t" \
    "v_smfmac_f32_32x32x32_bf16 v[16:31], v[108:111], v[56:63], v115\n\t" \
    "v_exp_f32 v67, v67\n\t" \
    "v_exp_f32 v68, v68\n\t" \
    "v_exp_f32 v69, v69\n\t" \
    "v_exp_f32 v70, v70\n\t" \
    "s_waitcnt lgkmcnt(0)\n\t" \
    "v_mfma_f32_32x32x16_bf16 v[48:63], v[88:91], v[80:83], 0\n\t" \
    "v_exp_f32 v71, v71\n\t" \
    "v_exp_f32 v72, v72\n\t" \
    "v_exp_f32 v73, v73\n\t" \
    "v_exp_f32 v74, v74\n\t" \
    "v_exp_f32 v75, v75\n\t" \
    "v_smfmac_f32_32x32x32_bf16 v[32:47], v[104:107], v[64:71], v115\n\t" \
    "v_exp_f32 v76, v76\n\t" \
    "v_exp_f32 v77, v77\n\t" \
    "v_add_u32 v112, 8192, v112\n\t" \
    "v_add_u32 v113, v114, v113\n\t" \
    "v_exp_f32 v78, v78\n\t" \
    "v_exp_f32 v79, v79\n\t" \
    "Lloop%=:\n\t" \
    "v_exp_f32 v48, v48\n\t" \
    "v_exp_f32 v49, v49\n\t" \
    "v_exp_f32 v50, v50\n\t" \
    "v_smfmac_f32_32x32x32_bf16 v[32:47], v[108:111], v[72:79], v115\n\t" \
    "v_exp_f32 v51, v51\n\t" \
    "ds_read_b128 v[92:95], v112 offset:2048\n\t" \
    "v_exp_f32 v52, v52\n\t" \
    "v_exp_f32 v53, v53\n\t" \
    "ds_read_b128 v[104:107], v113 offset:2048\n\t" \
    "v_exp_f32 v54, v54\n\t" \
    "v_mfma_f32_32x32x16_bf16 v[64:79], v[88:91], v[84:87], 0\n\t" \
    "v_exp_f32 v55, v55\n\t" \
    "v_exp_f32 v56, v56\n\t" \
    "ds_read_b128 v[108:111], v113 offset:2560\n\t" \
    "v_exp_f32 v57, v57\n\t" \
    "v_exp_f32 v58, v58\n\t" \
    "v_exp_f32 v59, v59\n\t" \
    "v_smfmac_f32_32x32x32_bf16 v[16:31], v[96:99], v[48:55], v115\n\t" \
    "v_exp_f32 v60, v60\n\t" \
    "v_exp_f32 v61, v61\n\t" \
    "v_exp_f32 v62, v62\n\t" \
    "v_exp_f32 v63, v63\n\t" \
    "v_exp_f32 v64, v64\n\t" \
    "v_exp_f32 v65, v65\n\t" \
    "v_exp_f32 v66, v66\n\t" \
    "v_smfmac_f32_32x32x32_bf16 v[16:31], v[100:103], v[56:63], v115\n\t" \
    "v_exp_f32 v67, v67\n\t" \
    "v_exp_f32 v68, v68\n\t" \
    "v_exp_f32 v69, v69\n\t" \
    "v_exp_f32 v70, v70\n\t" \
    "s_waitcnt lgkmcnt(0)\n\t" \
    "v_mfma_f32_32x32x16_bf16 v[48:63], v[92:95], v[80:83], 0\n\t" \
    "v_exp_f32 v71, v71\n\t" \
    "v_exp_f32 v72, v72\n\t" \
    "v_exp_f32 v73, v73\n\t" \
    "v_exp_f32 v74, v74\n\t" \
    "v_exp_f32 v75, v75\n\t" \
    "v_smfmac_f32_32x32x32_bf16 v[32:47], v[96:99], v[64:71], v115\n\t" \
    "v_exp_f32 v76, v76\n\t" \
    "v_exp_f32 v77, v77\n\t" \
    "v_exp_f32 v78, v78\n\t" \
    "v_exp_f32 v79, v79\n\t" \
    "v_exp_f32 v48, v48\n\t" \
    "v_exp_f32 v49, v49\n\t" \
    "v_exp_f32 v50, v50\n\t" \
    "v_smfmac_f32_32x32x32_bf16 v[32:47], v[100:103], v[72:79], v115\n\t" \
    "v_exp_f32 v51, v51\n\t" \
    "ds_read_b128 v[88:91], v112 offset:4096\n\t" \
    "v_exp_f32 v52, v52\n\t" \
    "v_exp_f32 v53, v53\n\t" \
    "ds_read_b128 v[96:99], v113 offset:4096\n\t" \
    "v_exp_f32 v54, v54\n\t" \
    "v_mfma_f32_32x32x16_bf16 v[64:79], v[92:95], v[84:87], 0\n\t" \
    "v_exp_f32 v55, v55\n\t" \
    "v_exp_f32 v56, v56\n\t" \
    "ds_read_b128 v[100:103], v113 offset:4608\n\t" \
    "v_exp_f32 v57, v57\n\t" \
    "v_exp_f32 v58, v58\n\t" \
    "v_exp_f32 v59, v59\n\t" \
    "v_smfmac_f32_32x32x32_bf16 v[16:31], v[104:107], v[48:55], v115\n\t" \
    "v_exp_f32 v60, v60\n\t" \
    "v_exp_f32 v61, v61\n\t" \
    "v_exp_f32 v62, v62\n\t" \
    "v_exp_f32 v63, v63\n\t" \
    "v_exp_f32 v64, v64\n\t" \
    "v_exp_f32 v65, v65\n\t" \
    "v_exp_f32 v66, v66\n\t" \
    "v_smfmac_f32_32x32x32_bf16 v[16:31], v[108:111], v[56:63], v115\n\t" \
    "v_exp_f32 v67, v67\n\t" \
    "v_exp_f32 v68, v68\n\t" \
    "v_exp_f32 v69, v69\n\t" \
    "v_exp_f32 v70, v70\n\t" \
    "s_waitcnt lgkmcnt(0)\n\t" \
    "v_mfma_f32_32x32x16_bf16 v[48:63], v[88:91], v[80:83], 0\n\t" \
    "v_exp_f32 v71, v71\n\t" \
    "v_exp_f32 v72, v72\n\t" \
    "v_exp_f32 v73, v73\n\t" \
    "v_exp_f32 v74, v74\n\t" \
    "v_exp_f32 v75, v75\n\t" \
    "v_smfmac_f32_32x32x32_bf16 v[32:47], v[104:107], v[64:71], v115\n\t" \
    "v_exp_f32 v76, v76\n\t" \
    "v_exp_f32 v77, v77\n\t" \
    "v_exp_f32 v78, v78\n\t" \
    "v_exp_f32 v79, v79\n\t" \
    "v_exp_f32 v48, v48\n\t" \
    "v_exp_f32 v49, v49\n\t" \
    "v_exp_f32 v50, v50\n\t" \
    "v_smfmac_f32_32x32x32_bf16 v[32:47], v[108:111], v[72:79], v115\n\t" \
    "v_exp_f32 v51, v51\n\t" \
    "ds_read_b128 v[92:95], v112 offset:6144\n\t" \
    "v_exp_f32 v52, v52\n\t" \
    "v_exp_f32 v53, v53\n\t" \
    "ds_read_b128 v[104:107], v113 offset:6144\n\t" \
    "v_exp_f32 v54, v54\n\t" \
    "v_mfma_f32_32x32x16_bf16 v[64:79], v[88:91], v[84:87], 0\n\t" \
    "v_exp_f32 v55, v55\n\t" \
    "v_exp_f32 v56, v56\n\t" \
    "ds_read_b128 v[108:111], v113 offset:6656\n\t" \
    "v_exp_f32 v57, v57\n\t" \
    "v_exp_f32 v58, v58\n\t" \
    "v_exp_f32 v59, v59\n\t" \
    "v_smfmac_f32_32x32x32_bf16 v[16:31], v[96:99], v[48:55], v115\n\t" \
    "v_exp_f32 v60, v60\n\t" \
    "v_exp_f32 v61, v61\n\t" \
    "v_exp_f32 v62, v62\n\t" \
    "v_exp_f32 v63, v63\n\t" \
    "v_exp_f32 v64, v64\n\t" \
    "v_exp_f32 v65, v65\n\t" \
    "v_exp_f32 v66, v66\n\t" \
    "v_smfmac_f32_32x32x32_bf16 v[16:31], v[100:103], v[56:63], v115\n\t" \
    "v_exp_f32 v67, v67\n\t" \
    "v_exp_f32 v68, v68\n\t" \
    "v_exp_f32 v69, v69\n\t" \
    "v_exp_f32 v70, v70\n\t" \
    "s_waitcnt lgkmcnt(0)\n\t" \
    "v_mfma_f32_32x32x16_bf16 v[48:63], v[92:95], v[80:83], 0\n\t" \
    "v_exp_f32 v71, v71\n\t" \
    "v_exp_f32 v72, v72\n\t" \
    "v_exp_f32 v73, v73\n\t" \
    "v_exp_f32 v74, v74\n\t" \
    "v_exp_f32 v75, v75\n\t" \
    "v_smfmac_f32_32x32x32_bf16 v[32:47], v[96:99], v[64:71], v115\n\t" \
    "v_exp_f32 v76, v76\n\t" \
    "v_exp_f32 v77, v77\n\t" \
    "v_exp_f32 v78, v78\n\t" \
    "v_exp_f32 v79, v79\n\t" \
    "v_exp_f32 v48, v48\n\t" \
    "v_exp_f32 v49, v49\n\t" \
    "v_exp_f32 v50, v50\n\t" \
    "v_smfmac_f32_32x32x32_bf16 v[32:47], v[100:103], v[72:79], v115\n\t" \
    "v_exp_f32 v51, v51\n\t" \
    "ds_read_b128 v[88:91], v112 offset:8192\n\t" \
    "v_exp_f32 v52, v52\n\t" \
    "v_exp_f32 v53, v53\n\t" \
    "ds_read_b128 v[96:99], v113 offset:8192\n\t" \
    "v_exp_f32 v54, v54\n\t" \
    "v_mfma_f32_32x32x16_bf16 v[64:79], v[92:95], v[84:87], 0\n\t" \
    "v_exp_f32 v55, v55\n\t" \
    "v_exp_f32 v56, v56\n\t" \
    "ds_read_b128 v[100:103], v113 offset:8704\n\t" \
    "v_exp_f32 v57, v57\n\t" \
    "v_exp_f32 v58, v58\n\t" \
    "v_exp_f32 v59, v59\n\t" \
    "v_smfmac_f32_32x32x32_bf16 v[16:31], v[104:107], v[48:55], v115\n\t" \
    "v_exp_f32 v60, v60\n\t" \
    "v_exp_f32 v61, v61\n\t" \
    "v_exp_f32 v62, v62\n\t" \
    "v_exp_f32 v63, v63\n\t" \
    "v_exp_f32 v64, v64\n\t" \
    "v_exp_f32 v65, v65\n\t" \
    "v_exp_f32 v66, v66\n\t" \
    "v_smfmac_f32_32x32x32_bf16 v[16:31], v[108:111], v[56:63], v115\n\t" \
    "v_exp_f32 v67, v67\n\t" \
    "v_exp_f32 v68, v68\n\t" \
    "v_exp_f32 v69, v69\n\t" \
    "v_exp_f32 v70, v70\n\t" \
    "s_waitcnt lgkmcnt(0)\n\t" \
    "v_mfma_f32_32x32x16_bf16 v[48:63], v[88:91], v[80:83], 0\n\t" \
    "v_exp_f32 v71, v71\n\t" \
    "v_exp_f32 v72, v72\n\t" \
    "v_exp_f32 v73, v73\n\t" \
    "v_exp_f32 v74, v74\n\t" \
    "v_exp_f32 v75, v75\n\t" \
    "v_smfmac_f32_32x32x32_bf16 v[32:47], v[104:107], v[64:71], v115\n\t" \
    "v_exp_f32 v76, v76\n\t" \
    "v_exp_f32 v77, v77\n\t" \
    "v_add_u32 v112, 8192, v112\n\t" \
    "v_add_u32 v113, v114, v113\n\t" \
    "v_exp_f32 v78, v78\n\t" \
    "v_exp_f32 v79, v79\n\t" \
    "s_sub_u32 %[cnt], %[cnt], 1\n\t" \
    "s_cmp_lg_u32 %[cnt], 0\n\t" \
    "s_cbranch_scc1 Lloop%=\n\t" \
    "s_nop 1\n\t" \
    "v_smfmac_f32_32x32x32_bf16 v[32:47], v[108:111], v[72:79], v115\n\t" \
    "s_nop 7\n\t" \
    "s_nop 7\n\t"
#define ATTN_LOOP_OPS : "+{v[16:31]}"(o0), "+{v[32:47]}"(o1), "+{v112}"(aK), "+{v113}"(aV), [cnt] "=&s"(cnt), "+{s[60:61]}"(gs0), "+{s64}"(gl0) : "{v[80:83]}"(q0), "{v[84:87]}"(q1), "{v114}"(inc), "{v115}"(0xDDDDDDDDu), [gvo] "v"(gvo), [ldr] "s"(ldr) : "memory", "scc", "v48", "v49", "v50", "v51", "v52", "v53", "v54", "v55", "v56", "v57", "v58", "v59", "v60", "v61", "v62", "v63", "v64", "v65", "v66", "v67", "v68", "v69", "v70", "v71", "v72", "v73", "v74", "v75", "v76", "v77", "v78", "v79", "v88", "v89", "v90", "v91", "v92", "v93", "v94", "v95", "v96", "v97", "v98", "v99", "v100", "v101", "v102", "v103", "v104", "v105", "v106", "v107", "v108", "v109", "v110", "v111"

struct QFrag { u32x4 w[4], x[4], g, xg; };
template <int C> DI void qfrag_read(QFrag& f, ldsp lds, int xoff, int woff, int goff, int hp) {
    const ldsp xb = lds + slot_of(C), wb = lds + slot_of(C) + 8192 + woff;
#pragma unroll
    for (int s = 0; s < 4; ++s) { f.w[s] = tr_read2(wb + s * 4096, wb + s * 4096 + 1024); f.x[s] = lds_b128(xb + (xoff ^ (s * 32))); }
    f.g = lds_b128(lds + goff + C * 128); f.xg = lds_b128(xb + (xoff ^ (hp * 32)));
}
DI void qfrag_mma(const QFrag& f, f32x16& acc, f32x16& zacc) {
#pragma unroll
    for (int s = 0; s < 4; ++s) acc = mfma32(f.w[s], f.x[s], acc);
    zacc = mfma32(f.g, f.xg, zacc);
}
#define SBAR() __builtin_amdgcn_sched_barrier(0)
template <int C0, int NC> DI void qproj_chunks(ldsp lds, int xoff, int woff, int goff, int hp, f32x16& acc, f32x16& zacc) {
    QFrag f0, f1;
    qfrag_read<C0>(f0, lds, xoff, woff, goff, hp); SBAR();
    if constexpr (NC > 1) { qfrag_read<C0 + 1>(f1, lds, xoff, woff, goff, hp); SBAR(); }
    qfrag_mma(f0, acc, zacc); SBAR();
    if constexpr (NC > 2) { qfrag_read<C0 + 2>(f0, lds, xoff, woff, goff, hp); SBAR(); }
    if constexpr (NC > 1) { qfrag_mma(f1, acc, zacc); SBAR(); }
    if constexpr (NC > 2) { qfrag_mma(f0, acc, zacc); SBAR(); }
}
DI unsigned lds_add_rtn(unsigned lds_addr) {
    unsigned old = 0;
    if ((threadIdx.x & 63) == 0) asm volatile("ds_add_rtn_u32 %0, %1, %2\n\ts_waitcnt lgkmcnt(0)" : "=&v"(old) : "v"(lds_addr), "v"(1u) : "memory");
    return __builtin_amdgcn_readfirstlane(old);
}
DI void lds_wait_ge(ldsp p, unsigned v) {
    _Pragma("nounroll") while (__builtin_amdgcn_readfirstlane(__hip_atomic_load((LAS unsigned*)p, RLX_WG)) < v) __builtin_amdgcn_s_sleep(2);
    asm volatile("" ::: "memory");
}
DI void wait_tags(GAS unsigned* flags, int l, unsigned tag) {
    _Pragma("nounroll") for (unsigned spin = 0; spin < SPIN_CAP; ++spin) {
        if (__all(__hip_atomic_load(flags + (l & 7), RLX_AGENT) == tag)) break;
        __builtin_amdgcn_s_sleep(1);
    }
}

__global__ __launch_bounds__(512, 2) void k_fused(const u16* __restrict__ x, const u16* __restrict__ wqA, const u16* __restrict__ wkA, const u16* __restrict__ wvA, const u16* __restrict__ woA,
                                                  const u16* __restrict__ wqB, const u16* __restrict__ wkB, const u16* __restrict__ wvB, const u16* __restrict__ woB,
                                                  const u16* __restrict__ gate_w, char* ws, u16* __restrict__ out) {
    extern __shared__ __attribute__((aligned(16))) char smem[];
    const ldsp lds = (ldsp)smem;
    const unsigned lds0 = (unsigned)(uintptr_t)lds;
    const int t = threadIdx.x, l = t & 63, hi = l >> 5, r32 = l & 31, i16 = l & 15, g4 = l >> 4, fh = g4 & 1;
    const int w = __builtin_amdgcn_readfirstlane(t >> 6);
    unsigned codepf;
    { unsigned long long pc;
      asm volatile("s_getpc_b64 %0" : "=s"(pc));
      asm volatile("global_load_dword %0, %1, %2" : "=v"(codepf) : "v"((w * 17 + min(l, 16)) * 128), "s"(pc) : "memory"); }
    const int xcd = blockIdx.x & 7, jb = blockIdx.x >> 3;
    const int rt = xcd * 4 + (jb >> 3), m = jb & 7, b = m >> 2, hg = m & 3;
    const int rg = w >> 2, hp = w & 3;
    const u16* wq = b ? wqB : wqA; const u16* wk = b ? wkB : wkA; const u16* wv = b ? wvB : wvA;

    unsigned epoch; u32x4 xk0, xk1;
    { const char* ep = ws + WS_EPOCH;
      asm volatile("global_load_dword %0, %1, %2 sc1" : "=v"(epoch) : "v"(0), "s"(ep) : "memory");
      const u16* xkp = x + (size_t)(rt * 64 + hg * 16) * DM + 64 * w;
      const unsigned xko = i16 * (DM * 2) + g4 * 16;
      asm volatile("global_load_dwordx4 %0, %1, %2" : "=v"(xk0) : "v"(xko), "s"(xkp) : "memory");
      asm volatile("global_load_dwordx4 %0, %1, %2 offset:64" : "=v"(xk1) : "v"(xko), "s"(xkp) : "memory"); }
    const unsigned l16 = l * 16;
    { const unsigned dst = lds0 + F_WKV + w * 4096;
      glds(wk + 64 * w * 16, l16, dst); glds(wk + 64 * w * 16 + 512, l16, dst + 1024); glds(wv + 64 * w * 16, l16, dst + 2048); glds(wv + 64 * w * 16 + 512, l16, dst + 3072); }
    asm volatile("s_mov_b32 m0, %2\n\ts_nop 0\n\tglobal_load_lds_dword %0, %1" :: "v"(l * 4), "s"(gate_w + hp * 128), "s"(lds0 + F_GW + hp * 256) : "memory");
    const u16* xsrc = x + (size_t)(rt * 64 + 8 * w) * DM;
    const unsigned xvo = (l >> 3) * (DM * 2) + (((l & 7) ^ (((8 * w + (l >> 3)) >> 1) & 7)) * 16);
    const u16* wsrc = wq + (size_t)(8 * w) * DM + hg * 128;
    const unsigned wvo = g4 * (DM * 2) + ((i16 ^ (g4 << 2)) * 16);
#define LD_C(c) do { glds(xsrc + (c) * 64, xvo, lds0 + slot_of(c) + w * 1024); \
        glds(wsrc + (size_t)(c) * 64 * DM, wvo, lds0 + slot_of(c) + 8192 + w * 2048); glds(wsrc + (size_t)((c) * 64 + 4) * DM, wvo, lds0 + slot_of(c) + 8192 + w * 2048 + 1024); } while (0)
    LD_C(0); LD_C(1);

    WAIT_VM(7);
    asm volatile("" : "+v"(epoch), "+v"(xk0), "+v"(xk1));
    asm volatile("" :: "v"(codepf));
    const unsigned tag = epoch + 1u;
    { const ldsp wb = lds + F_WKV + w * 4096 + (8 * g4 + (i16 >> 2)) * 32 + (i16 & 3) * 8;
      f32x4 d1 = {}, d2 = {};
      d1 = mfma16(tr_read2(wb, wb + 128), xk0, d1);
      d2 = mfma16(xk0, tr_read2(wb + 2048, wb + 2048 + 128), d2);
      d1 = mfma16(tr_read2(wb + 1024, wb + 1024 + 128), xk1, d1);
      d2 = mfma16(xk1, tr_read2(wb + 3072, wb + 3072 + 128), d2);
      *(LAS f32x4*)(lds + F_WKV + w * 4096 + l16) = d1; *(LAS f32x4*)(lds + F_WKV + w * 4096 + 1024 + l16) = d2; }
    if (w >= 2) { LD_C(2); LD_C(3); LD_C(4); WAIT_VM(9); } else WAIT_VM(0);
    LDS_BAR();
    float kvstat = 0.f;
    GAS u64* const kvflag = (GAS u64*)(ws + WS_KVFLAG) + b * 256;
    if (w < 2) {
        f32x4 a = {};
#pragma unroll
        for (int ww = 0; ww < 8; ++ww) a += lds_f128(lds + F_WKV + ww * 4096 + w * 1024 + l16);
        const unsigned p0 = cvtpk(a[0], a[1]), p1 = cvtpk(a[2], a[3]);
        char* img = ws + WS_KV + (size_t)b * 131072 + (2 * rt + (hg >> 1)) * 2048 + (g4 >> 1) * 8;
        const int f = hg & 1;
        const int off = w == 0 ? fh * 512 + (16 * f + swap23(i16)) * 16 : 1024 + f * 512 + fh * 256 + i16 * 16;
        __hip_atomic_store((GAS u64*)(img + off), ((u64)p1 << 32) | p0, RLX_AGENT);
        if (w == 0) { float kn = sq2(p0) + sq2(p1); kn += xor16(kn); kn += xor32(kn); kvstat = max16(kn); }
        else kvstat = max64(fmaxf(fmaxf(fabsf(bflo(p0)), fabsf(bfhi(p0))), fmaxf(fabsf(bflo(p1)), fabsf(bfhi(p1)))));
    }
    if (w < 2) { LD_C(2); LD_C(3); LD_C(4); }

    const int xrow = 32 * rg + r32;
    const int xoff = xrow * 128 + ((hi ^ ((xrow >> 1) & 7)) * 16);
    const int woff = (8 * hi + (i16 >> 2)) * 256 + (((4 * hp + 2 * fh + ((i16 & 3) >> 1)) ^ ((i16 >> 2) << 2)) * 16) + 8 * (i16 & 1);
    const int goff = F_GW + (16 * hp + 8 * hi) * 2;
    f32x16 acc = {}, zacc = {};
    qproj_chunks<0, 2>(lds, xoff, woff, goff, hp, acc, zacc);
    if (w < 2) {
        WAIT_VM(9);
        if (l == 0) __hip_atomic_store(kvflag + 2 * (4 * rt + hg) + w, ((u64)__float_as_uint(kvstat) << 32) | tag, RLX_AGENT);
        WAIT_VM(1);
    } else WAIT_VM(0);
    LDS_BAR();
    if (w < 7) { LD_C(5); LD_C(6); LD_C(7); }
#define LD_C7(c) do { glds(x + (size_t)(rt * 64 + 56) * DM + (c) * 64, xvo7, lds0 + slot_of(c) + 7 * 1024); \
        glds(wq + (size_t)((c) * 64 + 56) * DM + hg * 128, wvo, lds0 + slot_of(c) + 8192 + 7 * 2048); glds(wq + (size_t)((c) * 64 + 60) * DM + hg * 128, wvo, lds0 + slot_of(c) + 8192 + 7 * 2048 + 1024); } while (0)
    { const unsigned xvo7 = (l >> 3) * (DM * 2) + (((l & 7) ^ (4 + (l >> 4))) * 16);
      if (w == 4) LD_C7(5); else if (w == 5) LD_C7(6); else if (w == 6) LD_C7(7); }
    u32x4 fa, fb;
    const char* const flagp = (const char*)(ws + WS_KVFLAG) + b * 2048;
    if (w == 7 && l < 16) *(LAS unsigned*)(lds + M_CNT + l * 4) = 0u;
#define KV_LOOK() do { asm volatile("global_load_dwordx4 %0, %1, %2 sc1" : "=v"(fa) : "v"(l * 32), "s"(flagp) : "memory"); \
        asm volatile("global_load_dwordx4 %0, %1, %2 offset:16 sc1" : "=v"(fb) : "v"(l * 32), "s"(flagp) : "memory"); } while (0)
    qproj_chunks<2, 2>(lds, xoff, woff, goff, hp, acc, zacc);
    if (w == 7) {
        KV_LOOK();
        asm volatile("s_waitcnt vmcnt(0)" : "+v"(fa), "+v"(fb) :: "memory");
        _Pragma("nounroll") for (unsigned spin = 0; spin < SPIN_CAP; ++spin) {
            if (__all(fa[0] == tag && fa[2] == tag && fb[0] == tag && fb[2] == tag)) break;
            asm volatile("global_load_dwordx4 %0, %2, %3 sc1\n\tglobal_load_dwordx4 %1, %2, %3 offset:16 sc1\n\ts_waitcnt vmcnt(0)" : "=&v"(fa), "=&v"(fb) : "v"(l * 32), "s"(flagp) : "memory");
        }
        lds_max(lds0 + M_STAT, max(fa[1], fb[1])); lds_max(lds0 + M_STAT + 4, max(fa[3], fb[3]));
    }
    LDS_BAR();
    const char* const kvsrc = ws + WS_KV + (size_t)b * 131072;
#pragma unroll
    for (int i = 0; i < 3; ++i) glds_dev(kvsrc + i * 8192 + w * 1024, l16, lds0 + i * 8192 + w * 1024);
    const f32x2 stv = *(LAS const f32x2*)(lds + M_STAT);
    qproj_chunks<4, 1>(lds, xoff, woff, goff, hp, acc, zacc);
    WAIT_BAR(3);
    qproj_chunks<5, 3>(lds, xoff, woff, goff, hp, acc, zacc);
    LDS_BAR();

    u32x4 q0, q1;
#pragma unroll
    for (int i = 0; i < 4; ++i) { q0[i] = cvtpk(acc[2 * i] * QSCALE, acc[2 * i + 1] * QSCALE); q1[i] = cvtpk(acc[8 + 2 * i] * QSCALE, acc[9 + 2 * i] * QSCALE); }
    if (l < 32) *(LAS float*)(lds + M_Z + (w * 32 + l) * 4) = zacc[0];
    { const unsigned one = (l & 11) == 0 ? 0x3f803f80u : 0u;
      *(LAS u32x4*)(lds + M_ONES + t * 16) = (u32x4){one, one, one, one};
      if (t < 68) *(LAS u32x4*)(lds + M_ONES + 8192 + t * 16) = (u32x4){one, one, one, one}; }
    bool fast;
    { float n0 = 0.f, n1 = 0.f;
#pragma unroll
      for (int i = 0; i < 8; ++i) { n0 += acc[i] * acc[i]; n1 += acc[8 + i] * acc[8 + i]; }
      n0 += xor32(n0); n1 += xor32(n1);
      const float qn = fmaxf(n0, n1) * (1.02f * QSCALE * QSCALE);
      fast = __all(qn * stv[0] <= FAST_BOUND * FAST_BOUND && stv[1] <= 1e20f && stv[1] >= 1e-12f); }
    WAIT_BAR(0);
    float gz = 0.f;
#pragma unroll
    for (int i = 0; i < 4; ++i) gz += *(LAS const float*)(lds + M_Z + ((rg * 4 + i) * 32 + r32) * 4);

    __builtin_amdgcn_s_setprio(1);
    f32x16 o0 = {}, o1 = {};
    unsigned aK = lds0 + l16;
    const bool real = (l & 16) == 0;
    unsigned aV = lds0 + (real ? 1024 : M_ONES) + hi * 256 + i16 * 16;
    if (__builtin_amdgcn_readfirstlane((int)fast)) {
        unsigned cnt;
        const unsigned inc = real ? 8192u : 0u;
        const char* gs0 = kvsrc + 3 * 8192 + hp * 2048;
        unsigned gl0 = lds0 + 3 * 8192 + hp * 2048;
        const unsigned gvo = l16, ldr = (unsigned)(w >> 2) ^ 1u;
        asm volatile(ATTN_LOOP_ASM ATTN_LOOP_OPS);
    } else {
        if (w < 4) {
#pragma unroll
            for (int c = 3; c < 16; ++c) { const int off = c * 8192 + hp * 2048; glds_dev(kvsrc + off, l16, lds0 + off); glds_dev(kvsrc + off + 1024, l16, lds0 + off + 1024); }
        }
        WAIT_BAR(0);
        float m0 = -INFINITY, m1 = -INFINITY;
        const unsigned vstep = real ? 2048u : 0u;
        for (int s = 0; s < 64; ++s) {
            const u32x4 kf = lds_b128((ldsp)(uintptr_t)(aK + s * 2048));
            const u32x4 v0 = lds_b128((ldsp)(uintptr_t)(aV + s * vstep)), v1 = lds_b128((ldsp)(uintptr_t)(aV + s * vstep + 512));
#pragma unroll
            for (int tt = 0; tt < 2; ++tt) {
                f32x16& o = tt ? o1 : o0;
                float& mm = tt ? m1 : m0;
                f32x16 sc = mfma32(kf, tt ? q1 : q0, (f32x16){});
                float mx = sc[0];
#pragma unroll
                for (int r = 1; r < 16; ++r) mx = fmaxf(mx, sc[r]);
                mx = fmaxf(fmaxf(mx, xor32(mx)), mm);
                const float alpha = __builtin_amdgcn_exp2f(mm - mx);
                mm = mx;
                f32x8 p0, p1;
#pragma unroll
                for (int r = 0; r < 8; ++r) { p0[r] = __builtin_amdgcn_exp2f(sc[r] - mx); p1[r] = __builtin_amdgcn_exp2f(sc[8 + r] - mx); }
#pragma unroll
                for (int r = 0; r < 9; ++r) o[r] *= alpha;
                o = __builtin_amdgcn_smfmac_f32_32x32x32_bf16(__builtin_bit_cast(bf16x8, v0), __builtin_bit_cast(bf16x16, p0), o, (int)0xDDDDDDDDu, 0, 0);
                o = __builtin_amdgcn_smfmac_f32_32x32x32_bf16(__builtin_bit_cast(bf16x8, v1), __builtin_bit_cast(bf16x16, p1), o, (int)0xDDDDDDDDu, 0, 0);
            }
        }
    }
    if (w < 4) __builtin_amdgcn_s_setprio(0);

    char* const ofrag = ws + WS_O + (size_t)rt * 131072;
    { const float gate = 1.f / (1.f + __expf(-gz));
      const float gs = b ? 1.f - gate : gate;
      char* dst = ofrag + rg * 65536 + (8 * m + 2 * hp) * 1024 + l16;
#pragma unroll
      for (int tt = 0; tt < 2; ++tt) {
          const f32x16& o = tt ? o1 : o0;
          const float sc = gs * __builtin_amdgcn_rcpf(o[8]);
          store16_dev(dst + tt * 1024, (u32x4){cvtpk(o[0] * sc, o[1] * sc), cvtpk(o[2] * sc, o[3] * sc), cvtpk(o[4] * sc, o[5] * sc), cvtpk(o[6] * sc, o[7] * sc)});
      } }
    GAS unsigned* const oflag = (GAS unsigned*)(ws + WS_OFLAG) + rt * 64;
    const int cs = w & 1, kh = (w >> 1) & 1;
    u32x4 wf[32];
    auto out_store = [&](int half, f32x4 v0, f32x4 v1) {
        auto r0 = __builtin_amdgcn_permlane32_swap(cvtpk(v0[0], v0[1]), cvtpk(v1[0], v1[1]), false, false);
        auto r1 = __builtin_amdgcn_permlane32_swap(cvtpk(v0[2], v0[3]), cvtpk(v1[2], v1[3]), false, false);
        u16* p = out + (size_t)(rt * 64 + 32 * half + r32) * DM + m * 64 + cs * 32 + 16 * kh + 8 * hi;
        *(u32x4*)p = (u32x4){r0[0], r1[0], r0[1], r1[1]};
    };
#define ACC4(a, g) (f32x4){(a)[4 * (g)], (a)[4 * (g) + 1], (a)[4 * (g) + 2], (a)[4 * (g) + 3]}
    if (w < 4) {
        WAIT_VM(0);
        if (lds_add_rtn(lds0 + M_CNT) == 3u && l == 0) __hip_atomic_store(oflag + m, tag, RLX_AGENT);
        { const int dd = ((l >> 2) & 3) + 8 * ((l >> 4) & 1) + 4 * hi;
          const __amdgpu_buffer_rsrc_t wrsrc = __builtin_amdgcn_make_buffer_rsrc((void*)((kh ? woB : woA) + m * 64 + cs * 32), (short)0, DM * DM * 2 - (m * 64 + cs * 32) * 2, 0x00020000);
          const unsigned wovo = dd * (DM * 2) + (l & 3) * 16;
          u32x4 raw[32];
#pragma unroll
          for (int i = 0; i < 32; ++i) raw[i] = __builtin_amdgcn_raw_buffer_load_b128(wrsrc, wovo, i * 16 * DM * 2, 0);
          SBAR();
          const ldsp wb = lds + M_WO + w * 4096, wrd = wb + (8 * hi + (i16 >> 2)) * 64 + fh * 32 + (i16 & 3) * 8;
#pragma unroll
          for (int r = 0; r < 8; ++r) {
#pragma unroll
              for (int i = 0; i < 4; ++i) *(LAS u32x4*)(wb + i * 1024 + l16) = raw[4 * r + i];
#pragma unroll
              for (int s = 0; s < 4; ++s) wf[4 * r + s] = tr_read2(wrd + s * 1024, wrd + s * 1024 + 256);
#pragma unroll
              for (int s = 0; s < 4; ++s) asm volatile("" : "+v"(wf[4 * r + s]) :: "memory");
          } }
        if (w == 0) { wait_tags(oflag, l, tag); if (l == 0) __hip_atomic_store((LAS unsigned*)(lds + M_CNT + 4), 1u, RLX_WG); }
        lds_wait_ge(lds + M_CNT + 4, 1u);
        const __amdgpu_buffer_rsrc_t orsrc = __builtin_amdgcn_make_buffer_rsrc(ofrag + kh * 32768, (short)0, 32768, 0x00020000);
        f32x16 a0 = {};
        u32x4 f0[8], f1[8];
#define LD_F(f, bt) do { _Pragma("unroll") for (int i = 0; i < 8; ++i) f[i] = __builtin_amdgcn_raw_buffer_load_b128(orsrc, l16, (8 * (bt) + i) * 1024, 16); } while (0)
#define MM_F(f, bt) do { _Pragma("unroll") for (int i = 0; i < 8; ++i) a0 = mfma32(wf[8 * (bt) + i], f[i], a0); } while (0)
        LD_F(f0, 0); LD_F(f1, 1); SBAR();
        MM_F(f0, 0); SBAR(); LD_F(f0, 2); SBAR();
        MM_F(f1, 1); SBAR(); LD_F(f1, 3); SBAR();
        MM_F(f0, 2); MM_F(f1, 3); SBAR();
        const ldsp xb = lds + M_WO + w * 4096 + l16;
        *(LAS f32x4*)xb = kh ? ACC4(a0, 0) : ACC4(a0, 2); *(LAS f32x4*)(xb + 1024) = kh ? ACC4(a0, 1) : ACC4(a0, 3);
        asm volatile("s_waitcnt lgkmcnt(0)" ::: "memory");
        (void)lds_add_rtn(lds0 + M_CNT + 8 + 4 * cs);
        lds_wait_ge(lds + M_CNT + 8 + 4 * cs, 2u);
        const ldsp xr = lds + M_WO + (w ^ 2) * 4096 + l16;
        const f32x4 v0 = (kh ? ACC4(a0, 2) : ACC4(a0, 0)) + lds_f128(xr), v1 = (kh ? ACC4(a0, 3) : ACC4(a0, 1)) + lds_f128(xr + 1024);
        out_store(0, v0, v1);
    } else {
#pragma unroll
        for (int i = 0; i < 32; ++i) asm volatile("" : "=v"(wf[i]));
        WAIT_VM(0);
    }
    LDS_BAR();
    if (w == 4) {
        if (l == 0) __hip_atomic_store(oflag + 32 + m, tag, RLX_AGENT);
        wait_tags(oflag + 32, l, tag);
    }
    LDS_BAR();
    f32x16 a1 = {};
    if (w >= 4) {
        const char* osrc = ofrag + 65536 + hp * 2048;
#define LD_O(g) do { glds_dev(osrc + (g) * 8192, l16, lds0 + (g) * 8192 + hp * 2048); glds_dev(osrc + (g) * 8192 + 1024, l16, lds0 + (g) * 8192 + hp * 2048 + 1024); \
        glds_dev(osrc + 32768 + (g) * 8192, l16, lds0 + 32768 + (g) * 8192 + hp * 2048); glds_dev(osrc + 32768 + (g) * 8192 + 1024, l16, lds0 + 32768 + (g) * 8192 + hp * 2048 + 1024); } while (0)
        LD_O(0); LD_O(1); LD_O(2); LD_O(3);
        WAIT_BAR(8);
        WAIT_BAR(0);
    } else {
        const ldsp ob = lds + kh * 32768 + l16;
        u32x4 f[16];
        LDS_BAR();
#pragma unroll
        for (int i = 0; i < 16; ++i) f[i] = lds_b128(ob + i * 1024);
        SBAR();
#pragma unroll
        for (int i = 0; i < 16; ++i) a1 = mfma32(wf[i], f[i], a1);
        SBAR();
        LDS_BAR();
#pragma unroll
        for (int i = 0; i < 16; ++i) f[i] = lds_b128(ob + (16 + i) * 1024);
        SBAR();
#pragma unroll
        for (int i = 0; i < 16; ++i) a1 = mfma32(wf[16 + i], f[i], a1);
        SBAR();
    }
    if (w < 4) {
        const ldsp xb = lds + T_RED + w * 2048 + l16;
        *(LAS f32x4*)xb = kh ? ACC4(a1, 0) : ACC4(a1, 2); *(LAS f32x4*)(xb + 1024) = kh ? ACC4(a1, 1) : ACC4(a1, 3);
    }
    LDS_BAR();
    if (w < 4) {
        const ldsp xr = lds + T_RED + (w ^ 2) * 2048 + l16;
        const f32x4 v0 = (kh ? ACC4(a1, 2) : ACC4(a1, 0)) + lds_f128(xr), v1 = (kh ? ACC4(a1, 3) : ACC4(a1, 1)) + lds_f128(xr + 1024);
        out_store(1, v0, v1);
    } else if (blockIdx.x == 0 && w == 7) {
        GAS u64* const all = (GAS u64*)(ws + WS_KVFLAG);
        _Pragma("nounroll") for (unsigned spin = 0; spin < SPIN_CAP; ++spin) {
            bool ok = true;
#pragma unroll
            for (int i = 0; i < 4; ++i) ok &= (unsigned)__hip_atomic_load(all + 8 * l + 2 * i, RLX_AGENT) == tag;
            if (__all(ok)) break;
            __builtin_amdgcn_s_sleep(1);
        }
        if (l == 0) __hip_atomic_store((GAS unsigned*)(ws + WS_EPOCH), tag, RLX_AGENT);
    }
}

extern "C" void kernel_launch(void* const* d_in, const int* in_sizes, int n_in, void* d_out, int out_size, void* d_ws, size_t ws_size, hipStream_t stream) {
    (void)hipFuncSetAttribute((const void*)k_fused, hipFuncAttributeMaxDynamicSharedMemorySize, LDS_BYTES);
    k_fused<<<256, 512, LDS_BYTES, stream>>>((const u16*)d_in[0], (const u16*)d_in[1], (const u16*)d_in[2], (const u16*)d_in[3], (const u16*)d_in[4],
                                             (const u16*)d_in[6], (const u16*)d_in[7], (const u16*)d_in[8], (const u16*)d_in[9], (const u16*)d_in[10], (char*)d_ws, (u16*)d_out);
}
```
